```python
import torch

def _softmax(x, axis=-1):
    return torch.softmax(x, dim=axis)
def _gelu(x):
    return torch.nn.functional.gelu(x, approximate="tanh")
def _silu(x):
    return torch.nn.functional.silu(x)
def _sigmoid(x):
    return torch.sigmoid(x)
def _layernorm(x, axis=-1, eps=1e-5):
    m = x.mean(dim=axis, keepdim=True)
    v = x.var(dim=axis, keepdim=True, unbiased=False)
    return (x - m) / torch.sqrt(v + eps)
def _scatter_add(tgt, idx, src, axis=0):
    return tgt.clone().index_add_(axis, idx, src)
def _segment_sum(src, seg, n):
    out = torch.zeros((n,) + src.shape[1:], dtype=src.dtype)
    return out.index_add_(0, seg, src)
def _pad(x, pw):
    flat = tuple(v for pair in reversed(pw) for v in pair)
    return torch.nn.functional.pad(x, flat)

kernel_name = 'recurrent_scan__mha_block_cached__dense_activation_squared_relu__decode'

def setup_inputs():

    _w = (lambda a: (lambda t: t.to(torch.bfloat16) if t.is_floating_point() else t)(torch.as_tensor(a)))
    def _A_setup():
        B, Sq, D = 2, 1, 640
        H, Sk, Dh = 32, 8192, 128
        return {
            "x": _w((lambda *s: torch.randn(*s, dtype=torch.bfloat16) * 0.05)(B, Sq, D)),
            "k_cache": _w((lambda *s: torch.randn(*s, dtype=torch.bfloat16) * 0.8)(B, H, Sk, Dh)),
            "v_cache": _w((lambda *s: torch.randn(*s, dtype=torch.bfloat16) * 0.2)(B, H, Sk, Dh)),
            "wq": _w((lambda *s: torch.randn(*s, dtype=torch.bfloat16) * 0.8)(D, H * Dh) * (D ** -0.5)),
            "wo": _w((lambda *s: torch.randn(*s, dtype=torch.bfloat16) * 0.2)(H * Dh, D) * ((H * Dh) ** -0.5)),
        }
    _A_in = _A_setup()

    def _B_setup():
        B, S, D, F = 2, 1, 640, 1728
        return {
            "x": _w((lambda *s: torch.randn(*s, dtype=torch.bfloat16) * 0.05)(B, S, D)),
            "w1": _w((lambda *s: torch.randn(*s, dtype=torch.bfloat16) * 0.1)(D, F)),
            "b1": _w((lambda *s: torch.randn(*s, dtype=torch.bfloat16) * 0.1)(F)),
            "w2": _w((lambda *s: torch.randn(*s, dtype=torch.bfloat16) * 0.1)(F, D)),
            "b2": _w((lambda *s: torch.randn(*s, dtype=torch.bfloat16) * 0.1)(D)),
        }
    _B_in = _B_setup()

    return {
        "A_x": _A_in["x"],
        "A_k_cache": _A_in["k_cache"],
        "A_v_cache": _A_in["v_cache"],
        "A_wq": _A_in["wq"],
        "A_wo": _A_in["wo"],
        "B_x": _B_in["x"],
        "B_w1": _B_in["w1"],
        "B_b1": _B_in["b1"],
        "B_w2": _B_in["w2"],
        "B_b2": _B_in["b2"],
    }

def reference(A_x, A_k_cache, A_v_cache, A_wq, A_wo, B_x, B_w1, B_b1, B_w2, B_b2):
    def _A_ref(x, k_cache, v_cache, wq, wo):
        B, Sq, D = x.shape
        H, Dh = 32, 128
        q = (lambda x, *d: x.permute(*d))((x @ wq).reshape(B, Sq, H, Dh), 0, 2, 1, 3)
        scores = torch.einsum("bhqd,bhkd->bhqk", q, k_cache) * (Dh ** -0.5)
        p = _softmax(scores, axis=-1)
        o = torch.einsum("bhqk,bhkd->bhqd", p, v_cache)
        return (lambda x, *d: x.permute(*d))(o, 0, 2, 1, 3).reshape(B, Sq, H * Dh) @ wo

    def _B_ref(x, w1, b1, w2, b2):
        h = torch.einsum("bsd,df->bsf", x, w1) + b1
        zero = torch.zeros_like(h)
        r = torch.where(h > zero, h, zero)
        h = r * r
        out = torch.einsum("bsf,fd->bsd", h, w2) + b2
        return out

    x = A_x
    xn = _layernorm(x, axis=-1)
    s = _A_ref(xn, A_k_cache, A_v_cache, A_wq, A_wo)
    g = _silu(_B_ref(xn, B_w1, B_b1, B_w2, B_b2))
    return x + s * g
```

```cpp
#include <hip/hip_runtime.h>
#include <stdint.h>
#include <type_traits>

#define GAS __attribute__((address_space(1)))
#define LAS __attribute__((address_space(3)))
typedef unsigned v4u __attribute__((ext_vector_type(4)));
typedef unsigned v2u __attribute__((ext_vector_type(2)));
typedef float f32x4 __attribute__((ext_vector_type(4)));
typedef float f32x2 __attribute__((ext_vector_type(2)));
typedef __bf16 bf16x8 __attribute__((ext_vector_type(8)));
typedef __bf16 bf16x2 __attribute__((ext_vector_type(2)));
typedef __amdgpu_buffer_rsrc_t rsrc_t;

namespace {

constexpr int D = 640, NH = 32, SK = 8192, DH = 128, FF = 1728;
constexpr int NTHR = 512;
constexpr int NS = 6;
constexpr int NCH = 256;
constexpr int PF = 4;
constexpr int NPEEL = 10;
constexpr int E2 = 12;
constexpr int WOB = 2;
static_assert((PF + NPEEL + 1 + E2) * NS <= NCH && E2 % 2 == 0 && E2 >= 2, "the fixed tiles of every wave fit");
constexpr int NT = 2, PLAIN = 0, SC1 = 16;
constexpr float C2 = 0.08838834764831845f * 1.4426950408889634f;

constexpr int WS_EPOCH = 0;
constexpr int WS_ML = 4096;
constexpr int WS_F = 65536;
constexpr int WS_Y = 524288;
constexpr int WS_PB = WS_Y + 2 * 40 * 128 * 64;
constexpr int WS_BYTES = WS_PB + 27 * 8 * 1024;
constexpr unsigned SPIN_CAP = 1u << 20;

constexpr int L_XN = 0;
constexpr int L_QRED = 2560;
constexpr int L_ORED = 6656;
constexpr int L_ML = 9728;
constexpr int L_HV = 9792;
constexpr int L_CRED = 10304;
constexpr int L_YRED = 11328;
constexpr int L_BYTES = 31808;

__device__ __forceinline__ float bf_lo(unsigned w) { return __builtin_bit_cast(float, w << 16); }
__device__ __forceinline__ float bf_hi(unsigned w) { return __builtin_bit_cast(float, w & 0xffff0000u); }
__device__ __forceinline__ unsigned pk_bf16(float lo, float hi) {
  bf16x2 v = {(__bf16)lo, (__bf16)hi};
  return __builtin_bit_cast(unsigned, v);
}
__device__ __forceinline__ float rbf(float f) { return (float)(__bf16)f; }
template <int CTRL> __device__ __forceinline__ float dpp(float x) {
  return __builtin_bit_cast(float, __builtin_amdgcn_mov_dpp(__builtin_bit_cast(int, x), CTRL, 0xf, 0xf, true));
}
template <int CTRL> __device__ __forceinline__ unsigned dppu(unsigned x) {
  return (unsigned)__builtin_amdgcn_mov_dpp((int)x, CTRL, 0xf, 0xf, true);
}
constexpr int XOR1 = 0xB1, XOR2 = 0x4E, XOR7 = 0x141, XOR15 = 0x140, ROR4 = 0x124, ROR8 = 0x128;
__device__ __forceinline__ float x16_sum(float x) {
  auto s = __builtin_amdgcn_permlane16_swap(__float_as_uint(x), __float_as_uint(x), false, false);
  return __uint_as_float(s[0]) + __uint_as_float(s[1]);
}
__device__ __forceinline__ float x32_sum(float x) {
  auto s = __builtin_amdgcn_permlane32_swap(__float_as_uint(x), __float_as_uint(x), false, false);
  return __uint_as_float(s[0]) + __uint_as_float(s[1]);
}
__device__ __forceinline__ float x16_max(float x) {
  auto s = __builtin_amdgcn_permlane16_swap(__float_as_uint(x), __float_as_uint(x), false, false);
  return fmaxf(__uint_as_float(s[0]), __uint_as_float(s[1]));
}
__device__ __forceinline__ float x32_max(float x) {
  auto s = __builtin_amdgcn_permlane32_swap(__float_as_uint(x), __float_as_uint(x), false, false);
  return fmaxf(__uint_as_float(s[0]), __uint_as_float(s[1]));
}
__device__ __forceinline__ float wave_sum(float x) {
  x += dpp<XOR1>(x); x += dpp<XOR2>(x); x += dpp<XOR7>(x); x += dpp<XOR15>(x);
  return x32_sum(x16_sum(x));
}
__device__ __forceinline__ float dot2(unsigned a, unsigned b, float acc) {
  return __builtin_amdgcn_fdot2_f32_bf16(__builtin_bit_cast(bf16x2, a), __builtin_bit_cast(bf16x2, b), acc, false);
}
__device__ __forceinline__ f32x4 rowmac(unsigned P, const unsigned (&mk)[4], v4u rows, f32x4 acc) {
  const v4u a = {P & mk[0], P & mk[1], P & mk[2], P & mk[3]};
  return __builtin_amdgcn_mfma_f32_16x16x32_bf16(__builtin_bit_cast(bf16x8, a), __builtin_bit_cast(bf16x8, rows), acc, 0, 0, 0);
}
__device__ __forceinline__ rsrc_t mkrs(const void* p, unsigned bytes) { return __builtin_amdgcn_make_buffer_rsrc((void*)p, 0, bytes, 0x00020000); }
template <int AUX> __device__ __forceinline__ v4u ldx(rsrc_t r, unsigned voff, unsigned soff) { return __builtin_amdgcn_raw_buffer_load_b128(r, voff, soff, AUX); }
__device__ __forceinline__ v4u ld_sc1(rsrc_t r, unsigned off) { return __builtin_amdgcn_raw_buffer_load_b128(r, off, 0, SC1); }
__device__ __forceinline__ void st_sc1(rsrc_t r, unsigned off, v4u v) { __builtin_amdgcn_raw_buffer_store_b128(v, r, off, 0, SC1); }
__device__ __forceinline__ bool tagged(v4u v, unsigned T) { return v.y == T && v.w == T; }
__device__ __forceinline__ bool all_lanes(bool ok) { return __builtin_amdgcn_ballot_w64(!ok) == 0; }
#define SB() __builtin_amdgcn_sched_barrier(0)
#define REREAD() asm volatile("" ::: "memory")
#define DRAIN() do { SB(); __builtin_amdgcn_s_waitcnt(0x0F70); SB(); } while (0)

__device__ __forceinline__ unsigned tile_scores(const v4u (&k)[2], const unsigned (&q)[4], float& m, float& l, f32x4& acc, int lane) {
  float x[2];
#pragma unroll
  for (int i = 0; i < 2; ++i) {
    float d = dot2(k[i].x, q[0], 0.f);
    d = dot2(k[i].y, q[1], d); d = dot2(k[i].z, q[2], d); d = dot2(k[i].w, q[3], d);
    x[i] = d;
  }
  const float a0 = x[0] + dpp<XOR1>(x[0]), a1 = x[1] + dpp<XOR1>(x[1]);
  float s = (lane & 1) ? a1 : a0;
  s += dpp<XOR2>(s);
  s += dpp<ROR4>(s);
  s += dpp<ROR8>(s);
  s *= C2;
  if (__builtin_amdgcn_ballot_w64(s > m) != 0) {
    float mx = fmaxf(s, dpp<XOR1>(s));
    mx = x32_max(x16_max(mx));
    mx = fmaxf(mx, m);
    const float al = __builtin_amdgcn_exp2f(m - mx);
    l *= al; acc *= al; m = mx;
  }
  const float p = __builtin_amdgcn_exp2f(s - m);
  l += p;
  return pk_bf16(p, p);
}
__device__ __forceinline__ f32x4 tile_pv(unsigned P, const v4u (&v)[2], const unsigned (&mk)[4], f32x4 acc) {
  acc = rowmac(dppu<0x00>(P), mk, v[0], acc);
  acc = rowmac(dppu<0x55>(P), mk, v[1], acc);
  asm volatile("" : "+v"(acc));
  return acc;
}

__global__ void __launch_bounds__(NTHR, 2)
fused_decode_kernel(const unsigned short* __restrict__ x, const unsigned short* __restrict__ kc, const unsigned short* __restrict__ vc,
                    const unsigned short* __restrict__ wq, const unsigned short* __restrict__ wo, const unsigned short* __restrict__ w1,
                    const unsigned short* __restrict__ b1, const unsigned short* __restrict__ w2, const unsigned short* __restrict__ b2,
                    unsigned short* __restrict__ out, unsigned char* __restrict__ ws) {
  asm volatile("" :: "s"(x), "s"(kc), "s"(vc), "s"(wq), "s"(wo), "s"(w1), "s"(b1), "s"(w2), "s"(b2), "s"(out), "s"(ws));
  __shared__ __attribute__((aligned(16))) unsigned char smem[L_BYTES];
  LAS unsigned char* L = (LAS unsigned char*)smem;
  const int tid = threadIdx.x, lane = tid & 63, w = __builtin_amdgcn_readfirstlane(tid >> 6);
  const int c = blockIdx.x, xl = c & 7, ii = c >> 3;
  const int h = xl * 4 + (ii >> 3), j8 = ii & 7, b = j8 >> 2, s = j8 & 3;
  const int c16 = lane & 15, g = lane >> 4;
  const rsrc_t rs = mkrs(ws, WS_BYTES);
  const rsrc_t rK = mkrs(kc, 2u * NH * SK * DH * 2), rV = mkrs(vc, 2u * NH * SK * DH * 2);
  const rsrc_t rQ = mkrs(wq, D * NH * DH * 2), rO = mkrs(wo, D * NH * DH * 2);

  const unsigned epoch = __builtin_amdgcn_raw_buffer_load_b32(rs, WS_EPOCH, 0, SC1);
  unsigned xw[5];
  {
    const GAS unsigned* x32 = (const GAS unsigned*)x + (w >> 2) * 320;
#pragma unroll
    for (int k = 0; k < 5; ++k) xw[k] = x32[k * 64 + lane];
  }
  SB();
  const int G = xl + 8 * (ii >> 3);
  const bool ffn = G < 27;
  const int uo = c >= 176 ? c - 176 : 0, bo = uo / 40, ch = uo - 40 * bo;
  SB();
  const unsigned kvb = ((unsigned)(b * NH + h) * SK + s * 2048) * 256u;
  const unsigned lo16 = lane * 16;
  const int rot = j8 % 5;
  const unsigned wqb = (80 * w) * 8192 + h * 256, wqoff = g * (20 * 8192) + c16 * 16;
  v4u wreg[20];
  v4u pk[PF][2], pv[PF][2];
  unsigned mk[4];
#pragma unroll
  for (int jj = 0; jj < 4; ++jj) mk[jj] = (c16 < 8 && (c16 >> 1) == jj) ? ((c16 & 1) ? 0xffff0000u : 0x0000ffffu) : 0u;
  unsigned T = 0;
#define ROTK(k) ((k) + rot >= 5 ? (k) + rot - 5 : (k) + rot)
#define WQ_GROUP(k) do { const unsigned rb_ = wqb + ROTK(k) * (4 * 8192);                              \
    _Pragma("unroll") for (int i_ = 0; i_ < 4; ++i_) wreg[4 * (k) + i_] = ldx<PLAIN>(rQ, wqoff, rb_ + i_ * 8192); } while (0)
#define K_LOADS(buf, ck) _Pragma("unroll") for (int i_ = 0; i_ < 2; ++i_) pk[buf][i_] = ldx<NT>(rK, lo16 + i_ * 1024, kvb + (ck) * 2048)
#define V_LOADS(buf, ck) _Pragma("unroll") for (int i_ = 0; i_ < 2; ++i_) pv[buf][i_] = ldx<NT>(rV, lo16 + i_ * 1024, kvb + (ck) * 2048)
#define KV_PRE(n) do { if constexpr (ST) { K_LOADS(n, (n) * NS + w); V_LOADS(n, (n) * NS + w); } } while (0)
#define GROUP_END() do { SB(); __builtin_amdgcn_s_barrier(); SB(); } while (0)
  auto q_phase = [&](auto st_, auto helper_first_loads) __attribute__((always_inline)) {
    constexpr bool ST = decltype(st_)::value;
    if constexpr (ST) { WQ_GROUP(0); WQ_GROUP(1); } else helper_first_loads();
    SB();
    {
      asm volatile("" : "+v"(xw[0]), "+v"(xw[1]), "+v"(xw[2]), "+v"(xw[3]), "+v"(xw[4]));
      float v[10];
#pragma unroll
      for (int k = 0; k < 5; ++k) { v[2 * k] = bf_lo(xw[k]); v[2 * k + 1] = bf_hi(xw[k]); }
      const float x0 = __builtin_bit_cast(float, __builtin_amdgcn_readfirstlane(__builtin_bit_cast(int, v[0])));
      float s1 = 0.f, s2 = 0.f;
#pragma unroll
      for (int k = 0; k < 10; ++k) { const float d = v[k] - x0; s1 += d; s2 = fmaf(d, d, s2); }
      s1 = wave_sum(s1) * (1.0f / D); s2 = wave_sum(s2) * (1.0f / D);
      const float mean = x0 + s1, var = fmaxf(fmaf(-s1, s1, s2), 0.f);
      const float mb = rbf(mean);
      const float den = rbf(__builtin_amdgcn_sqrtf(rbf(rbf(var) + 1e-5f)));
      const float inv = __builtin_amdgcn_rcpf(den);
      LAS unsigned* xn32 = (LAS unsigned*)(L + L_XN) + (w >> 2) * 320;
#pragma unroll
      for (int k = 0; k < 5; ++k) xn32[k * 64 + lane] = pk_bf16(rbf(v[2 * k] - mb) * inv, rbf(v[2 * k + 1] - mb) * inv);
      asm volatile("s_waitcnt lgkmcnt(0)" ::: "memory");
    }
    GROUP_END();
    KV_PRE(0); KV_PRE(1);
    if constexpr (!ST) { WQ_GROUP(0); WQ_GROUP(1); }
    WQ_GROUP(2); WQ_GROUP(3); WQ_GROUP(4);
    GROUP_END();
    static_assert(PF == 4, "prologue issue order");
    KV_PRE(2); KV_PRE(3);
    SB();
    T = __builtin_amdgcn_readfirstlane(epoch) + 1u;
    const LAS v2u* xp = (const LAS v2u*)(L + L_XN + b * 1280 + (80 * w + 20 * g) * 2);
    f32x4 qa = {0.f, 0.f, 0.f, 0.f}, qb = {0.f, 0.f, 0.f, 0.f};
#pragma unroll
    for (int k = 0; k < 5; ++k) {
      const v2u r = xp[ROTK(k)];
      qa = rowmac(__builtin_amdgcn_perm(r.x, r.x, 0x01000100u), mk, wreg[4 * k], qa);
      qb = rowmac(__builtin_amdgcn_perm(r.x, r.x, 0x03020302u), mk, wreg[4 * k + 1], qb);
      qa = rowmac(__builtin_amdgcn_perm(r.y, r.y, 0x01000100u), mk, wreg[4 * k + 2], qa);
      qb = rowmac(__builtin_amdgcn_perm(r.y, r.y, 0x03020302u), mk, wreg[4 * k + 3], qb);
    }
    qa += qb;
    if (lane < 32) *(LAS f32x4*)(L + L_QRED + (w * 128 + 8 * c16 + 4 * g) * 4) = qa;
  };

  const unsigned wob = (h * 128 + 16 * w) * 1280;
  const unsigned wooff = g * (4 * 1280) + c16 * 16;
#define WO_LOAD(n) wreg[n] = ldx<PLAIN>(rO, wooff + ((n) % 5) * 256, wob + ((n) / 5) * 1280)
  f32x4 fo[2] = {{0.f, 0.f, 0.f, 0.f}, {0.f, 0.f, 0.f, 0.f}};

  if (w < NS) {
    q_phase(std::true_type{}, [] {});
    __syncthreads();
    unsigned q[4];
    {
      const LAS f32x4* qr = (const LAS f32x4*)(L + L_QRED + ((2 * g) * 128 + 8 * c16) * 4);
      f32x4 lo = qr[0] + qr[32], hi = qr[1] + qr[33];
      float e[8] = {lo.x, lo.y, lo.z, lo.w, hi.x, hi.y, hi.z, hi.w};
#pragma unroll
      for (int k = 0; k < 8; ++k) e[k] = x32_sum(x16_sum(e[k]));
#pragma unroll
      for (int k = 0; k < 4; ++k) q[k] = pk_bf16(e[2 * k], e[2 * k + 1]);
    }
    SB();
    float m = -INFINITY, l = 0.f;
    f32x4 acc = {0.f, 0.f, 0.f, 0.f};
    unsigned nx = PF * NS + w;
#define TILE_STEP(buf, EXTRA) do { const unsigned P_ = tile_scores(pk[buf], q, m, l, acc, lane); SB(); K_LOADS(buf, nx); EXTRA; SB();  \
    acc = tile_pv(P_, pv[buf], mk, acc); SB(); V_LOADS(buf, nx); SB(); nx += NS; } while (0)
#define TILE_LAST(buf) do { const unsigned P_ = tile_scores(pk[buf], q, m, l, acc, lane); acc = tile_pv(P_, pv[buf], mk, acc); SB(); } while (0)
    TILE_STEP(0, );
#pragma unroll
    for (int u = 1; u < PF; ++u) TILE_LAST(u);
#pragma unroll
    for (int u = 0; u < NPEEL; ++u) TILE_STEP(0, WO_LOAD(2 * u); WO_LOAD(2 * u + 1));
    const unsigned nlim = ((NCH - w + NS - 1) / NS - (E2 - 1)) * NS + w;
#pragma nounroll
    while (nx < nlim) TILE_STEP(0, );
    K_LOADS(1, nx); V_LOADS(1, nx);
    SB();
    nx += NS;
#pragma nounroll
    for (int j = 0; j < E2 / 2 - 1; ++j) { TILE_STEP(0, ); TILE_STEP(1, ); }
    TILE_LAST(0);
    TILE_LAST(1);
#undef TILE_STEP
#undef TILE_LAST
    const float lw = x32_sum(x16_sum(l + dpp<XOR1>(l)));
    if (lane < 32) *(LAS f32x4*)(L + L_ORED + (w * 128 + 8 * c16 + 4 * g) * 4) = acc;
    if (lane == 0) *(LAS f32x2*)(L + L_ML + w * 8) = f32x2{m, lw};
  } else {
    v4u fw[16];
    const bool hA = w == NS;
    const rsrc_t rF = mkrs(hA ? w1 : w2, ffn ? D * FF * 2 : 0), rF2 = mkrs(w2, ffn && !hA ? D * FF * 2 : 0);
    const unsigned fvoff = hA ? (10 * (2 * g + (c16 >> 3))) * 3456 + (c16 & 7) * 16 : g * (16 * 1280) + (c16 < 10 ? c16 : 9) * 16;
    const unsigned fsoff = hA ? (80 * j8) * 3456 + 128 * G : (64 * G) * 1280 + 160 * j8;
    q_phase(std::false_type{}, [&]() __attribute__((always_inline)) {
#pragma unroll
      for (int k = 0; k < 16; ++k) fw[k] = ldx<PLAIN>(k < 10 ? rF : rF2, fvoff, fsoff + (hA ? k * 3456 : k * 1280));
    });
    __syncthreads();
    const int fu = 8 * (lane & 7) + 4 * ((lane >> 3) & 1) + 2 * ((lane >> 4) & 1);
    const unsigned b1v = ((const GAS unsigned*)b1)[(ffn ? 32 * G : 0) + (fu >> 1)];
    if (hA && ffn) {
      unsigned mk2[4];
#pragma unroll
      for (int jj = 0; jj < 4; ++jj) mk2[jj] = ((c16 & 7) >> 1) == jj ? ((c16 & 1) ? 0xffff0000u : 0x0000ffffu) : 0u;
      const LAS unsigned* xs = (const LAS unsigned*)(L + L_XN + 160 * j8 + 20 * (2 * g + (c16 >> 3)));
      f32x4 d0 = {0.f, 0.f, 0.f, 0.f}, d1 = {0.f, 0.f, 0.f, 0.f};
#pragma unroll
      for (int k = 0; k < 10; ++k) {
        const unsigned x0_ = xs[k >> 1], x1_ = xs[320 + (k >> 1)], sel = (k & 1) ? 0x03020302u : 0x01000100u;
        d0 = rowmac(__builtin_amdgcn_perm(x0_, x0_, sel), mk2, fw[k], d0);
        d1 = rowmac(__builtin_amdgcn_perm(x1_, x1_, sel), mk2, fw[k], d1);
      }
#pragma unroll
      for (int bb = 0; bb < 2; ++bb) {
        f32x4 d = bb ? d1 : d0;
#pragma unroll
        for (int r = 0; r < 4; ++r) {
          auto sw = __builtin_amdgcn_permlane32_swap(__float_as_uint(d[r]), __float_as_uint(d[r]), false, false);
          d[r] += dpp<ROR8>(__uint_as_float(sw[1]));
        }
        const unsigned o = WS_PB + (G * 8 + j8) * 1024 + bb * 512 + (c16 + 8 * g) * 16;
        if (lane < 32 && c16 < 8) {
          st_sc1(rs, o, v4u{__float_as_uint(d.x), T, __float_as_uint(d.y), T});
          st_sc1(rs, o + 256, v4u{__float_as_uint(d.z), T, __float_as_uint(d.w), T});
        }
      }
    }
#pragma unroll
    for (int n = 0; n < 20; n += WOB) {
#pragma unroll
      for (int k = n; k < n + WOB; ++k) WO_LOAD(k);
      DRAIN();
    }
    if (!hA && ffn) {
      v4u pg[8];
#pragma nounroll
      for (unsigned spins = 0; spins < SPIN_CAP; ++spins) {
        REREAD();
        bool ok = true;
#pragma unroll
        for (int k = 0; k < 8; ++k) pg[k] = ld_sc1(rs, WS_PB + (G * 8 + k) * 1024 + lane * 16);
#pragma unroll
        for (int k = 0; k < 8; ++k) ok = ok && tagged(pg[k], T);
        if (all_lanes(ok)) break;
        __builtin_amdgcn_s_sleep(32);
      }
      float a0 = 0.f, a1 = 0.f;
#pragma unroll
      for (int k = 0; k < 8; ++k) { a0 += __uint_as_float(pg[k].x); a1 += __uint_as_float(pg[k].z); }
      const float r0 = fmaxf(rbf(rbf(a0) + bf_lo(b1v)), 0.f), r1 = fmaxf(rbf(rbf(a1) + bf_hi(b1v)), 0.f);
      const float h0 = r0 * r0, h1 = r1 * r1;
      *(LAS v2u*)(L + L_HV + ((lane >> 5) * 64 + fu) * 4) = v2u{pk_bf16(h0, h0), pk_bf16(h1, h1)};
      asm volatile("s_waitcnt lgkmcnt(0)" ::: "memory");
#pragma unroll
      for (int bb = 0; bb < 2; ++bb) {
        f32x4 da = {0.f, 0.f, 0.f, 0.f}, db = {0.f, 0.f, 0.f, 0.f};
#pragma unroll
        for (int k = 0; k < 4; ++k) {
          const v4u hv = *(const LAS v4u*)(L + L_HV + (bb * 64 + 16 * g + 4 * k) * 4);
          da = rowmac(hv.x, mk, fw[4 * k], da); db = rowmac(hv.y, mk, fw[4 * k + 1], db);
          da = rowmac(hv.z, mk, fw[4 * k + 2], da); db = rowmac(hv.w, mk, fw[4 * k + 3], db);
        }
        fo[bb] = da + db;
      }
    }
  }
#undef WO_LOAD

  DRAIN();
  __syncthreads();
  if (w == NS + 1 && ffn && lane < 32 && c16 < 10) {
#pragma unroll
    for (int bb = 0; bb < 2; ++bb) {
      const unsigned o = WS_F + ((bb * 40 + 5 * j8 + (c16 >> 1)) * 32 + G) * 128 + (2 * (c16 & 1) + g) * 16;
      st_sc1(rs, o, v4u{__float_as_uint(fo[bb].x), T, __float_as_uint(fo[bb].y), T});
      st_sc1(rs, o + 64, v4u{__float_as_uint(fo[bb].z), T, __float_as_uint(fo[bb].w), T});
    }
  }
  const int p = h * 4 + s;
  {
    f32x2 ml[NS];
    f32x4 ov[NS];
#pragma unroll
    for (int ww = 0; ww < NS; ++ww) ml[ww] = *(const LAS f32x2*)(L + L_ML + ww * 8);
#pragma unroll
    for (int ww = 0; ww < NS; ++ww) ov[ww] = *(const LAS f32x4*)(L + L_ORED + (ww * 128 + 16 * w + 4 * g) * 4);
    SB();
    float M = ml[0].x;
#pragma unroll
    for (int ww = 1; ww < NS; ++ww) M = fmaxf(M, ml[ww].x);
    float Ls = 0.f;
    f32x4 o = {0.f, 0.f, 0.f, 0.f};
#pragma unroll
    for (int ww = 0; ww < NS; ++ww) {
      const float f = __builtin_amdgcn_exp2f(ml[ww].x - M);
      Ls = fmaf(f, ml[ww].y, Ls);
      o += f * ov[ww];
    }
    if (tid == 448) st_sc1(rs, WS_ML + (b * 128 + p) * 16, v4u{__float_as_uint(M), T, __float_as_uint(Ls), T});
    f32x4 y[5];
#pragma unroll
    for (int cc = 0; cc < 5; ++cc) y[cc] = f32x4{0.f, 0.f, 0.f, 0.f};
#pragma unroll
    for (int i = 0; i < 4; ++i) {
      const unsigned P = pk_bf16(o[i], o[i]);
#pragma unroll
      for (int cc = 0; cc < 5; ++cc) y[cc] = rowmac(P, mk, wreg[i * 5 + cc], y[cc]);
    }
    if (lane < 32) {
#pragma unroll
      for (int cc = 0; cc < 5; ++cc) *(LAS f32x4*)(L + L_YRED + (w * 640 + cc * 128 + 8 * c16 + 4 * g) * 4) = y[cc];
    }
  }
  __syncthreads();
  if (tid < 160) {
    f32x4 a = {0.f, 0.f, 0.f, 0.f};
#pragma unroll
    for (int ww = 0; ww < 8; ++ww) a += *(const LAS f32x4*)(L + L_YRED + (ww * 640 + 4 * tid) * 4);
    st_sc1(rs, WS_Y + ((b * 40 + (tid >> 2)) * 128 + p) * 64 + (tid & 3) * 16, v4u{pk_bf16(a.x, a.y), T, pk_bf16(a.z, a.w), T});
  }
  if (c < 176) return;

  const unsigned short xov = x[bo * D + 16 * ch + c16], b2v = b2[16 * ch + c16];
  SB();
  const int pa = 16 * w + (lane >> 2), pc = lane & 7, kf = 4 * w + (lane >> 3);
  const bool fv = lane < 32 && kf < 27;
  const unsigned foff = WS_F + ((bo * 40 + ch) * 32 + (fv ? kf : 0)) * 128 + pc * 16;
  v4u ma, fa;
#pragma nounroll
  for (unsigned spins = 0; spins < SPIN_CAP; ++spins) {
    REREAD();
    ma = ld_sc1(rs, WS_ML + (bo * 128 + pa) * 16);
    fa = ld_sc1(rs, foff);
    if (all_lanes(tagged(ma, T) && tagged(fa, T))) break;
    __builtin_amdgcn_s_sleep(1);
  }
  const unsigned yoff = WS_Y + ((bo * 40 + ch) * 128 + pa) * 64 + (lane & 3) * 16;
  v4u ya = ld_sc1(rs, yoff);
  SB();
  float fA;
  {
    const float m_ = __uint_as_float(ma.x), l_ = __uint_as_float(ma.z);
    float M = fmaxf(m_, dpp<ROR4>(m_));
    M = fmaxf(M, dpp<ROR8>(M));
    const float e = __builtin_amdgcn_exp2f(m_ - M);
    float Ls = e * l_;
    Ls += dpp<ROR4>(Ls);
    Ls += dpp<ROR8>(Ls);
    fA = e / Ls;
  }
  float r2[2] = {fv ? __uint_as_float(fa.x) : 0.f, fv ? __uint_as_float(fa.z) : 0.f};
#pragma unroll
  for (int k = 0; k < 2; ++k) { r2[k] += dpp<ROR8>(r2[k]); r2[k] = x32_sum(x16_sum(r2[k])); }
  if (lane < 8) *(LAS f32x2*)(L + L_CRED + (w * 32 + 16 + 4 * (pc & 3) + 2 * (pc >> 2)) * 4) = f32x2{r2[0], r2[1]};
  SB();
  if (!all_lanes(tagged(ya, T))) {
#pragma nounroll
    for (unsigned spins = 0; spins < SPIN_CAP; ++spins) {
      REREAD();
      ya = ld_sc1(rs, yoff);
      if (all_lanes(tagged(ya, T))) break;
    }
  }
  f32x4 r4 = {fA * bf_lo(ya.x), fA * bf_hi(ya.x), fA * bf_lo(ya.z), fA * bf_hi(ya.z)};
#pragma unroll
  for (int k = 0; k < 4; ++k) { float t_ = r4[k]; t_ += dpp<ROR4>(t_); t_ += dpp<ROR8>(t_); r4[k] = x32_sum(x16_sum(t_)); }
  if (lane < 4) *(LAS f32x4*)(L + L_CRED + (w * 32 + 4 * lane) * 4) = r4;
  __syncthreads();
  if (tid < 16) {
    float sv = 0.f, fv2 = 0.f;
#pragma unroll
    for (int ww = 0; ww < 8; ++ww) {
      sv += *(const LAS float*)(L + L_CRED + (ww * 32 + tid) * 4);
      fv2 += *(const LAS float*)(L + L_CRED + (ww * 32 + 16 + tid) * 4);
    }
    const float t = rbf(rbf(fv2) + bf_lo(b2v));
    const float gt = rbf(t / (1.0f + __expf(-t)));
    const float o = bf_lo(xov) + rbf(rbf(sv) * gt);
    out[bo * D + 16 * ch + tid] = __builtin_bit_cast(unsigned short, (__bf16)o);
  }
  if (uo == 0 && w == 1) {
#pragma nounroll
    for (unsigned spins = 0; spins < SPIN_CAP; ++spins) {
      REREAD();
      const v4u a = ld_sc1(rs, WS_ML + (128 + lane) * 16);
      const v4u d = ld_sc1(rs, WS_ML + (192 + lane) * 16);
      if (all_lanes(tagged(a, T) && tagged(d, T))) break;
      __builtin_amdgcn_s_sleep(1);
    }
    if (lane == 0) __builtin_amdgcn_raw_buffer_store_b32(T, rs, WS_EPOCH, 0, SC1);
  }
}

}

extern "C" void kernel_launch(void* const* d_in, const int* in_sizes, int n_in, void* d_out, int out_size, void* d_ws, size_t ws_size,
                              hipStream_t stream) {
  (void)in_sizes; (void)n_in; (void)out_size; (void)ws_size;
  fused_decode_kernel<<<256, NTHR, 0, stream>>>(
      (const unsigned short*)d_in[0], (const unsigned short*)d_in[1], (const unsigned short*)d_in[2], (const unsigned short*)d_in[3],
      (const unsigned short*)d_in[4], (const unsigned short*)d_in[6], (const unsigned short*)d_in[7], (const unsigned short*)d_in[8],
      (const unsigned short*)d_in[9], (unsigned short*)d_out, (unsigned char*)d_ws);
}
```

```cpp
#include <hip/hip_runtime.h>
#include <stdint.h>
#include <type_traits>

#define GAS __attribute__((address_space(1)))
#define LAS __attribute__((address_space(3)))
typedef unsigned v4u __attribute__((ext_vector_type(4)));
typedef unsigned v2u __attribute__((ext_vector_type(2)));
typedef float f32x4 __attribute__((ext_vector_type(4)));
typedef float f32x2 __attribute__((ext_vector_type(2)));
typedef __bf16 bf16x8 __attribute__((ext_vector_type(8)));
typedef __bf16 bf16x2 __attribute__((ext_vector_type(2)));
typedef __amdgpu_buffer_rsrc_t rsrc_t;

namespace {

constexpr int D = 640, NH = 32, SK = 8192, DH = 128, FF = 1728;
constexpr int NTHR = 512;
constexpr int NS = 6;
constexpr int NCH = 256;
constexpr int PF = 4;
constexpr int NPEEL = 10;
constexpr int E2_EVEN = 8, E2_ODD = 14;
constexpr int WOB = 2;
static_assert((PF + NPEEL + 1 + E2_ODD) * NS <= NCH && E2_EVEN >= 2 && E2_EVEN % 2 == 0 && E2_ODD % 2 == 0, "the fixed tiles fit");
constexpr int NT = 2, PLAIN = 0, SC1 = 16;
constexpr float SCALE = 0.08838834764831845f, LOG2E = 1.4426950408889634f;

constexpr int WS_EPOCH = 0;
constexpr int WS_ML = 4096;
constexpr int WS_F = 65536;
constexpr int WS_Y = 524288;
constexpr int WS_PB = WS_Y + 2 * 40 * 128 * 64;
constexpr int WS_BYTES = WS_PB + 27 * 8 * 1024;
constexpr unsigned SPIN_CAP = 1u << 20;

constexpr int L_XN = 0;
constexpr int L_QRED = 2560;
constexpr int L_ORED = 6656;
constexpr int L_ML = 9728;
constexpr int L_HV = 9792;
constexpr int L_CRED = 10304;
constexpr int L_YRED = 11328;
constexpr int L_BYTES = 31808;

__device__ __forceinline__ float bf_lo(unsigned w) { return __builtin_bit_cast(float, w << 16); }
__device__ __forceinline__ float bf_hi(unsigned w) { return __builtin_bit_cast(float, w & 0xffff0000u); }
__device__ __forceinline__ unsigned pk_bf16(float lo, float hi) {
  bf16x2 v = {(__bf16)lo, (__bf16)hi};
  return __builtin_bit_cast(unsigned, v);
}
__device__ __forceinline__ float rbf(float f) { return (float)(__bf16)f; }
template <int CTRL> __device__ __forceinline__ float dpp(float x) {
  return __builtin_bit_cast(float, __builtin_amdgcn_mov_dpp(__builtin_bit_cast(int, x), CTRL, 0xf, 0xf, true));
}
template <int CTRL> __device__ __forceinline__ unsigned dppu(unsigned x) {
  return (unsigned)__builtin_amdgcn_mov_dpp((int)x, CTRL, 0xf, 0xf, true);
}
constexpr int XOR1 = 0xB1, XOR2 = 0x4E, XOR7 = 0x141, XOR15 = 0x140, ROR4 = 0x124, ROR8 = 0x128;
__device__ __forceinline__ float x16_sum(float x) {
  auto s = __builtin_amdgcn_permlane16_swap(__float_as_uint(x), __float_as_uint(x), false, false);
  return __uint_as_float(s[0]) + __uint_as_float(s[1]);
}
__device__ __forceinline__ float x32_sum(float x) {
  auto s = __builtin_amdgcn_permlane32_swap(__float_as_uint(x), __float_as_uint(x), false, false);
  return __uint_as_float(s[0]) + __uint_as_float(s[1]);
}
__device__ __forceinline__ float x16_max(float x) {
  auto s = __builtin_amdgcn_permlane16_swap(__float_as_uint(x), __float_as_uint(x), false, false);
  return fmaxf(__uint_as_float(s[0]), __uint_as_float(s[1]));
}
__device__ __forceinline__ float x32_max(float x) {
  auto s = __builtin_amdgcn_permlane32_swap(__float_as_uint(x), __float_as_uint(x), false, false);
  return fmaxf(__uint_as_float(s[0]), __uint_as_float(s[1]));
}
__device__ __forceinline__ float wave_sum(float x) {
  x += dpp<XOR1>(x); x += dpp<XOR2>(x); x += dpp<XOR7>(x); x += dpp<XOR15>(x);
  return x32_sum(x16_sum(x));
}
__device__ __forceinline__ float dot2(unsigned a, unsigned b, float acc) {
  return __builtin_amdgcn_fdot2_f32_bf16(__builtin_bit_cast(bf16x2, a), __builtin_bit_cast(bf16x2, b), acc, false);
}
__device__ __forceinline__ f32x4 rowmac(unsigned P, const unsigned (&mk)[4], v4u rows, f32x4 acc) {
  const v4u a = {P & mk[0], P & mk[1], P & mk[2], P & mk[3]};
  return __builtin_amdgcn_mfma_f32_16x16x32_bf16(__builtin_bit_cast(bf16x8, a), __builtin_bit_cast(bf16x8, rows), acc, 0, 0, 0);
}
__device__ __forceinline__ rsrc_t mkrs(const void* p, unsigned bytes) { return __builtin_amdgcn_make_buffer_rsrc((void*)p, 0, bytes, 0x00020000); }
template <int AUX> __device__ __forceinline__ v4u ldx(rsrc_t r, unsigned voff, unsigned soff) { return __builtin_amdgcn_raw_buffer_load_b128(r, voff, soff, AUX); }
__device__ __forceinline__ v4u ld_sc1(rsrc_t r, unsigned off) { return __builtin_amdgcn_raw_buffer_load_b128(r, off, 0, SC1); }
__device__ __forceinline__ void st_sc1(rsrc_t r, unsigned off, v4u v) { __builtin_amdgcn_raw_buffer_store_b128(v, r, off, 0, SC1); }
__device__ __forceinline__ bool tagged(v4u v, unsigned T) { return v.y == T && v.w == T; }
__device__ __forceinline__ bool all_lanes(bool ok) { return __builtin_amdgcn_ballot_w64(!ok) == 0; }
#define SB() __builtin_amdgcn_sched_barrier(0)
#define REREAD() asm volatile("" ::: "memory")
#define DRAIN() do { SB(); __builtin_amdgcn_s_waitcnt(0x0F70); SB(); } while (0)

__device__ __forceinline__ unsigned tile_scores(const v4u (&k)[2], const unsigned (&q)[4], float& m, float& l, f32x4& acc, int lane) {
  float x[2];
#pragma unroll
  for (int i = 0; i < 2; ++i) {
    float d = dot2(k[i].x, q[0], 0.f);
    d = dot2(k[i].y, q[1], d); d = dot2(k[i].z, q[2], d); d = dot2(k[i].w, q[3], d);
    x[i] = d;
  }
  const float a0 = x[0] + dpp<XOR1>(x[0]), a1 = x[1] + dpp<XOR1>(x[1]);
  float s = (lane & 1) ? a1 : a0;
  s += dpp<XOR2>(s);
  s += dpp<ROR4>(s);
  s += dpp<ROR8>(s);
  s = rbf(rbf(s) * SCALE) * LOG2E;
  if (__builtin_amdgcn_ballot_w64(s > m) != 0) {
    float mx = fmaxf(s, dpp<XOR1>(s));
    mx = x32_max(x16_max(mx));
    mx = fmaxf(mx, m);
    const float al = __builtin_amdgcn_exp2f(m - mx);
    l *= al; acc *= al; m = mx;
  }
  const float p = __builtin_amdgcn_exp2f(s - m);
  l += p;
  return pk_bf16(p, p);
}
__device__ __forceinline__ f32x4 tile_pv(unsigned P, const v4u (&v)[2], const unsigned (&mk)[4], f32x4 acc) {
  acc = rowmac(dppu<0x00>(P), mk, v[0], acc);
  acc = rowmac(dppu<0x55>(P), mk, v[1], acc);
  asm volatile("" : "+v"(acc));
  return acc;
}

__global__ void __launch_bounds__(NTHR, 2)
fused_decode_kernel(const unsigned short* __restrict__ x, const unsigned short* __restrict__ kc, const unsigned short* __restrict__ vc,
                    const unsigned short* __restrict__ wq, const unsigned short* __restrict__ wo, const unsigned short* __restrict__ w1,
                    const unsigned short* __restrict__ b1, const unsigned short* __restrict__ w2, const unsigned short* __restrict__ b2,
                    unsigned short* __restrict__ out, unsigned char* __restrict__ ws) {
  asm volatile("" :: "s"(x), "s"(kc), "s"(vc), "s"(wq), "s"(wo), "s"(w1), "s"(b1), "s"(w2), "s"(b2), "s"(out), "s"(ws));
  __shared__ __attribute__((aligned(16))) unsigned char smem[L_BYTES];
  LAS unsigned char* L = (LAS unsigned char*)smem;
  const int tid = threadIdx.x, lane = tid & 63, w = __builtin_amdgcn_readfirstlane(tid >> 6);
  const int c = blockIdx.x, xl = c & 7, ii = c >> 3;
  const int h = xl * 4 + (ii >> 3), j8 = ii & 7, b = j8 >> 2, s = j8 & 3;
  const int c16 = lane & 15, g = lane >> 4;
  const rsrc_t rs = mkrs(ws, WS_BYTES);
  const rsrc_t rK = mkrs(kc, 2u * NH * SK * DH * 2), rV = mkrs(vc, 2u * NH * SK * DH * 2);
  const rsrc_t rQ = mkrs(wq, D * NH * DH * 2), rO = mkrs(wo, D * NH * DH * 2);

  const unsigned epoch = __builtin_amdgcn_raw_buffer_load_b32(rs, WS_EPOCH, 0, SC1);
  unsigned xw[5];
  {
    const GAS unsigned* x32 = (const GAS unsigned*)x + (w >> 2) * 320;
#pragma unroll
    for (int k = 0; k < 5; ++k) xw[k] = x32[k * 64 + lane];
  }
  SB();
  const int G = xl + 8 * (ii >> 3);
  const bool ffn = G < 27;
  const int uo = c >= 176 ? c - 176 : 0, bo = uo / 40, ch = uo - 40 * bo;
  SB();
  const unsigned kvb = ((unsigned)(b * NH + h) * SK + s * 2048) * 256u;
  const unsigned lo16 = lane * 16;
  const int rot = j8 % 5;
  const unsigned wqb = (80 * w) * 8192 + h * 256, wqoff = g * (20 * 8192) + c16 * 16;
  v4u wreg[20];
  v4u pk[PF][2], pv[PF][2];
  unsigned mk[4];
#pragma unroll
  for (int jj = 0; jj < 4; ++jj) mk[jj] = (c16 < 8 && (c16 >> 1) == jj) ? ((c16 & 1) ? 0xffff0000u : 0x0000ffffu) : 0u;
  unsigned T = 0;
#define ROTK(k) ((k) + rot >= 5 ? (k) + rot - 5 : (k) + rot)
#define WQ_GROUP(k) do { const unsigned rb_ = wqb + ROTK(k) * (4 * 8192);                              \
    _Pragma("unroll") for (int i_ = 0; i_ < 4; ++i_) wreg[4 * (k) + i_] = ldx<PLAIN>(rQ, wqoff, rb_ + i_ * 8192); } while (0)
#define K_LOADS(buf, ck) _Pragma("unroll") for (int i_ = 0; i_ < 2; ++i_) pk[buf][i_] = ldx<NT>(rK, lo16 + i_ * 1024, kvb + (ck) * 2048)
#define V_LOADS(buf, ck) _Pragma("unroll") for (int i_ = 0; i_ < 2; ++i_) pv[buf][i_] = ldx<NT>(rV, lo16 + i_ * 1024, kvb + (ck) * 2048)
#define KV_PRE(n) do { if constexpr (ST) { K_LOADS(n, (n) * NS + w); V_LOADS(n, (n) * NS + w); } } while (0)
#define GROUP_END() do { SB(); __builtin_amdgcn_s_barrier(); SB(); } while (0)
  auto q_phase = [&](auto st_, auto helper_first_loads) __attribute__((always_inline)) {
    constexpr bool ST = decltype(st_)::value;
    if constexpr (ST) { WQ_GROUP(0); WQ_GROUP(1); } else helper_first_loads();
    SB();
    {
      asm volatile("" : "+v"(xw[0]), "+v"(xw[1]), "+v"(xw[2]), "+v"(xw[3]), "+v"(xw[4]));
      float v[10];
#pragma unroll
      for (int k = 0; k < 5; ++k) { v[2 * k] = bf_lo(xw[k]); v[2 * k + 1] = bf_hi(xw[k]); }
      const float x0 = __builtin_bit_cast(float, __builtin_amdgcn_readfirstlane(__builtin_bit_cast(int, v[0])));
      float s1 = 0.f, s2 = 0.f;
#pragma unroll
      for (int k = 0; k < 10; ++k) { const float d = v[k] - x0; s1 += d; s2 = fmaf(d, d, s2); }
      s1 = wave_sum(s1) * (1.0f / D); s2 = wave_sum(s2) * (1.0f / D);
      const float mean = x0 + s1, var = fmaxf(fmaf(-s1, s1, s2), 0.f);
      const float mb = rbf(mean);
      const float den = rbf(__builtin_amdgcn_sqrtf(rbf(rbf(var) + 1e-5f)));
      const float inv = __builtin_amdgcn_rcpf(den);
      LAS unsigned* xn32 = (LAS unsigned*)(L + L_XN) + (w >> 2) * 320;
#pragma unroll
      for (int k = 0; k < 5; ++k) xn32[k * 64 + lane] = pk_bf16(rbf(v[2 * k] - mb) * inv, rbf(v[2 * k + 1] - mb) * inv);
      asm volatile("s_waitcnt lgkmcnt(0)" ::: "memory");
    }
    GROUP_END();
    KV_PRE(0); KV_PRE(1);
    if constexpr (!ST) { WQ_GROUP(0); WQ_GROUP(1); }
    WQ_GROUP(2); WQ_GROUP(3); WQ_GROUP(4);
    GROUP_END();
    static_assert(PF == 4, "prologue issue order");
    KV_PRE(2); KV_PRE(3);
    SB();
    T = __builtin_amdgcn_readfirstlane(epoch) + 1u;
    const LAS v2u* xp = (const LAS v2u*)(L + L_XN + b * 1280 + (80 * w + 20 * g) * 2);
    f32x4 qa = {0.f, 0.f, 0.f, 0.f}, qb = {0.f, 0.f, 0.f, 0.f};
#pragma unroll
    for (int k = 0; k < 5; ++k) {
      const v2u r = xp[ROTK(k)];
      qa = rowmac(__builtin_amdgcn_perm(r.x, r.x, 0x01000100u), mk, wreg[4 * k], qa);
      qb = rowmac(__builtin_amdgcn_perm(r.x, r.x, 0x03020302u), mk, wreg[4 * k + 1], qb);
      qa = rowmac(__builtin_amdgcn_perm(r.y, r.y, 0x01000100u), mk, wreg[4 * k + 2], qa);
      qb = rowmac(__builtin_amdgcn_perm(r.y, r.y, 0x03020302u), mk, wreg[4 * k + 3], qb);
    }
    qa += qb;
    if (lane < 32) *(LAS f32x4*)(L + L_QRED + (w * 128 + 8 * c16 + 4 * g) * 4) = qa;
  };

  const unsigned wob = (h * 128 + 16 * w) * 1280;
  const unsigned wooff = g * (4 * 1280) + c16 * 16;
#define WO_LOAD(n) wreg[n] = ldx<PLAIN>(rO, wooff + ((n) % 5) * 256, wob + ((n) / 5) * 1280)
  f32x4 fo[2] = {{0.f, 0.f, 0.f, 0.f}, {0.f, 0.f, 0.f, 0.f}};

  if (w < NS) {
    q_phase(std::true_type{}, [] {});
    __syncthreads();
    unsigned q[4];
    {
      const LAS f32x4* qr = (const LAS f32x4*)(L + L_QRED + ((2 * g) * 128 + 8 * c16) * 4);
      f32x4 lo = qr[0] + qr[32], hi = qr[1] + qr[33];
      float e[8] = {lo.x, lo.y, lo.z, lo.w, hi.x, hi.y, hi.z, hi.w};
#pragma unroll
      for (int k = 0; k < 8; ++k) e[k] = x32_sum(x16_sum(e[k]));
#pragma unroll
      for (int k = 0; k < 4; ++k) q[k] = pk_bf16(e[2 * k], e[2 * k + 1]);
    }
    SB();
    float m = -INFINITY, l = 0.f;
    f32x4 acc = {0.f, 0.f, 0.f, 0.f};
    unsigned nx = PF * NS + w;
#define TILE_STEP(buf, EXTRA) do { const unsigned P_ = tile_scores(pk[buf], q, m, l, acc, lane); SB(); K_LOADS(buf, nx); EXTRA; SB();  \
    acc = tile_pv(P_, pv[buf], mk, acc); SB(); V_LOADS(buf, nx); SB(); nx += NS; } while (0)
#define TILE_LAST(buf) do { const unsigned P_ = tile_scores(pk[buf], q, m, l, acc, lane); acc = tile_pv(P_, pv[buf], mk, acc); SB(); } while (0)
    TILE_STEP(0, );
#pragma unroll
    for (int u = 1; u < PF; ++u) TILE_LAST(u);
#pragma unroll
    for (int u = 0; u < NPEEL; ++u) TILE_STEP(0, WO_LOAD(2 * u); WO_LOAD(2 * u + 1));
    const int e2 = (__builtin_amdgcn_s_getreg(20 | (3 << 11)) & 1) ? E2_ODD : E2_EVEN;
    const unsigned nlim = ((NCH - w + NS - 1) / NS - (e2 - 1)) * NS + w;
#pragma nounroll
    while (nx < nlim) TILE_STEP(0, );
    K_LOADS(1, nx); V_LOADS(1, nx);
    SB();
    nx += NS;
#pragma nounroll
    for (int j = 0; j < e2 / 2 - 1; ++j) { TILE_STEP(0, ); TILE_STEP(1, ); }
    TILE_LAST(0);
    TILE_LAST(1);
#undef TILE_STEP
#undef TILE_LAST
    const float lw = x32_sum(x16_sum(l + dpp<XOR1>(l)));
    if (lane < 32) *(LAS f32x4*)(L + L_ORED + (w * 128 + 8 * c16 + 4 * g) * 4) = acc;
    if (lane == 0) *(LAS f32x2*)(L + L_ML + w * 8) = f32x2{m, lw};
  } else {
    v4u fw[16];
    const bool hA = w == NS;
    const rsrc_t rF = mkrs(hA ? w1 : w2, ffn ? D * FF * 2 : 0), rF2 = mkrs(w2, ffn && !hA ? D * FF * 2 : 0);
    const unsigned fvoff = hA ? (10 * (2 * g + (c16 >> 3))) * 3456 + (c16 & 7) * 16 : g * (16 * 1280) + (c16 < 10 ? c16 : 9) * 16;
    const unsigned fsoff = hA ? (80 * j8) * 3456 + 128 * G : (64 * G) * 1280 + 160 * j8;
    q_phase(std::false_type{}, [&]() __attribute__((always_inline)) {
#pragma unroll
      for (int k = 0; k < 16; ++k) fw[k] = ldx<PLAIN>(k < 10 ? rF : rF2, fvoff, fsoff + (hA ? k * 3456 : k * 1280));
    });
    __syncthreads();
    const int fu = 8 * (lane & 7) + 4 * ((lane >> 3) & 1) + 2 * ((lane >> 4) & 1);
    const unsigned b1v = ((const GAS unsigned*)b1)[(ffn ? 32 * G : 0) + (fu >> 1)];
    if (hA && ffn) {
      unsigned mk2[4];
#pragma unroll
      for (int jj = 0; jj < 4; ++jj) mk2[jj] = ((c16 & 7) >> 1) == jj ? ((c16 & 1) ? 0xffff0000u : 0x0000ffffu) : 0u;
      const LAS unsigned* xs = (const LAS unsigned*)(L + L_XN + 160 * j8 + 20 * (2 * g + (c16 >> 3)));
      f32x4 d0 = {0.f, 0.f, 0.f, 0.f}, d1 = {0.f, 0.f, 0.f, 0.f};
#pragma unroll
      for (int k = 0; k < 10; ++k) {
        const unsigned x0_ = xs[k >> 1], x1_ = xs[320 + (k >> 1)], sel = (k & 1) ? 0x03020302u : 0x01000100u;
        d0 = rowmac(__builtin_amdgcn_perm(x0_, x0_, sel), mk2, fw[k], d0);
        d1 = rowmac(__builtin_amdgcn_perm(x1_, x1_, sel), mk2, fw[k], d1);
      }
#pragma unroll
      for (int bb = 0; bb < 2; ++bb) {
        f32x4 d = bb ? d1 : d0;
#pragma unroll
        for (int r = 0; r < 4; ++r) {
          auto sw = __builtin_amdgcn_permlane32_swap(__float_as_uint(d[r]), __float_as_uint(d[r]), false, false);
          d[r] += dpp<ROR8>(__uint_as_float(sw[1]));
        }
        const unsigned o = WS_PB + (G * 8 + j8) * 1024 + bb * 512 + (c16 + 8 * g) * 16;
        if (lane < 32 && c16 < 8) {
          st_sc1(rs, o, v4u{__float_as_uint(d.x), T, __float_as_uint(d.y), T});
          st_sc1(rs, o + 256, v4u{__float_as_uint(d.z), T, __float_as_uint(d.w), T});
        }
      }
    }
#pragma unroll
    for (int n = 0; n < 20; n += WOB) {
#pragma unroll
      for (int k = n; k < n + WOB; ++k) WO_LOAD(k);
      DRAIN();
    }
    if (!hA && ffn) {
      v4u pg[8];
#pragma nounroll
      for (unsigned spins = 0; spins < SPIN_CAP; ++spins) {
        REREAD();
        bool ok = true;
#pragma unroll
        for (int k = 0; k < 8; ++k) pg[k] = ld_sc1(rs, WS_PB + (G * 8 + k) * 1024 + lane * 16);
#pragma unroll
        for (int k = 0; k < 8; ++k) ok = ok && tagged(pg[k], T);
        if (all_lanes(ok)) break;
        __builtin_amdgcn_s_sleep(32);
      }
      float a0 = 0.f, a1 = 0.f;
#pragma unroll
      for (int k = 0; k < 8; ++k) { a0 += __uint_as_float(pg[k].x); a1 += __uint_as_float(pg[k].z); }
      const float r0 = fmaxf(rbf(rbf(a0) + bf_lo(b1v)), 0.f), r1 = fmaxf(rbf(rbf(a1) + bf_hi(b1v)), 0.f);
      const float h0 = r0 * r0, h1 = r1 * r1;
      *(LAS v2u*)(L + L_HV + ((lane >> 5) * 64 + fu) * 4) = v2u{pk_bf16(h0, h0), pk_bf16(h1, h1)};
      asm volatile("s_waitcnt lgkmcnt(0)" ::: "memory");
#pragma unroll
      for (int bb = 0; bb < 2; ++bb) {
        f32x4 da = {0.f, 0.f, 0.f, 0.f}, db = {0.f, 0.f, 0.f, 0.f};
#pragma unroll
        for (int k = 0; k < 4; ++k) {
          const v4u hv = *(const LAS v4u*)(L + L_HV + (bb * 64 + 16 * g + 4 * k) * 4);
          da = rowmac(hv.x, mk, fw[4 * k], da); db = rowmac(hv.y, mk, fw[4 * k + 1], db);
          da = rowmac(hv.z, mk, fw[4 * k + 2], da); db = rowmac(hv.w, mk, fw[4 * k + 3], db);
        }
        fo[bb] = da + db;
      }
    }
  }
#undef WO_LOAD

  DRAIN();
  __syncthreads();
  if (w == NS + 1 && ffn && lane < 32 && c16 < 10) {
#pragma unroll
    for (int bb = 0; bb < 2; ++bb) {
      const unsigned o = WS_F + ((bb * 40 + 5 * j8 + (c16 >> 1)) * 32 + G) * 128 + (2 * (c16 & 1) + g) * 16;
      st_sc1(rs, o, v4u{__float_as_uint(fo[bb].x), T, __float_as_uint(fo[bb].y), T});
      st_sc1(rs, o + 64, v4u{__float_as_uint(fo[bb].z), T, __float_as_uint(fo[bb].w), T});
    }
  }
  const int p = h * 4 + s;
  {
    f32x2 ml[NS];
    f32x4 ov[NS];
#pragma unroll
    for (int ww = 0; ww < NS; ++ww) ml[ww] = *(const LAS f32x2*)(L + L_ML + ww * 8);
#pragma unroll
    for (int ww = 0; ww < NS; ++ww) ov[ww] = *(const LAS f32x4*)(L + L_ORED + (ww * 128 + 16 * w + 4 * g) * 4);
    SB();
    float M = ml[0].x;
#pragma unroll
    for (int ww = 1; ww < NS; ++ww) M = fmaxf(M, ml[ww].x);
    float Ls = 0.f;
    f32x4 o = {0.f, 0.f, 0.f, 0.f};
#pragma unroll
    for (int ww = 0; ww < NS; ++ww) {
      const float f = __builtin_amdgcn_exp2f(ml[ww].x - M);
      Ls = fmaf(f, ml[ww].y, Ls);
      o += f * ov[ww];
    }
    if (tid == 448) st_sc1(rs, WS_ML + (b * 128 + p) * 16, v4u{__float_as_uint(M), T, __float_as_uint(Ls), T});
    f32x4 y[5];
#pragma unroll
    for (int cc = 0; cc < 5; ++cc) y[cc] = f32x4{0.f, 0.f, 0.f, 0.f};
#pragma unroll
    for (int i = 0; i < 4; ++i) {
      const unsigned P = pk_bf16(o[i], o[i]);
#pragma unroll
      for (int cc = 0; cc < 5; ++cc) y[cc] = rowmac(P, mk, wreg[i * 5 + cc], y[cc]);
    }
    if (lane < 32) {
#pragma unroll
      for (int cc = 0; cc < 5; ++cc) *(LAS f32x4*)(L + L_YRED + (w * 640 + cc * 128 + 8 * c16 + 4 * g) * 4) = y[cc];
    }
  }
  __syncthreads();
  if (tid < 160) {
    f32x4 a = {0.f, 0.f, 0.f, 0.f};
#pragma unroll
    for (int ww = 0; ww < 8; ++ww) a += *(const LAS f32x4*)(L + L_YRED + (ww * 640 + 4 * tid) * 4);
    st_sc1(rs, WS_Y + ((b * 40 + (tid >> 2)) * 128 + p) * 64 + (tid & 3) * 16, v4u{pk_bf16(a.x, a.y), T, pk_bf16(a.z, a.w), T});
  }
  if (c < 176) return;

  const unsigned short xov = x[bo * D + 16 * ch + c16], b2v = b2[16 * ch + c16];
  SB();
  const int pa = 16 * w + (lane >> 2), pc = lane & 7, kf = 4 * w + (lane >> 3);
  const bool fv = lane < 32 && kf < 27;
  const unsigned foff = WS_F + ((bo * 40 + ch) * 32 + (fv ? kf : 0)) * 128 + pc * 16;
  v4u ma, fa;
#pragma nounroll
  for (unsigned spins = 0; spins < SPIN_CAP; ++spins) {
    REREAD();
    ma = ld_sc1(rs, WS_ML + (bo * 128 + pa) * 16);
    fa = ld_sc1(rs, foff);
    if (all_lanes(tagged(ma, T) && tagged(fa, T))) break;
    __builtin_amdgcn_s_sleep(1);
  }
  const unsigned yoff = WS_Y + ((bo * 40 + ch) * 128 + pa) * 64 + (lane & 3) * 16;
  v4u ya = ld_sc1(rs, yoff);
  SB();
  float fA;
  {
    const float m_ = __uint_as_float(ma.x), l_ = __uint_as_float(ma.z);
    float M = fmaxf(m_, dpp<ROR4>(m_));
    M = fmaxf(M, dpp<ROR8>(M));
    const float e = __builtin_amdgcn_exp2f(m_ - M);
    float Ls = e * l_;
    Ls += dpp<ROR4>(Ls);
    Ls += dpp<ROR8>(Ls);
    fA = e / Ls;
  }
  float r2[2] = {fv ? __uint_as_float(fa.x) : 0.f, fv ? __uint_as_float(fa.z) : 0.f};
#pragma unroll
  for (int k = 0; k < 2; ++k) { r2[k] += dpp<ROR8>(r2[k]); r2[k] = x32_sum(x16_sum(r2[k])); }
  if (lane < 8) *(LAS f32x2*)(L + L_CRED + (w * 32 + 16 + 4 * (pc & 3) + 2 * (pc >> 2)) * 4) = f32x2{r2[0], r2[1]};
  SB();
  if (!all_lanes(tagged(ya, T))) {
#pragma nounroll
    for (unsigned spins = 0; spins < SPIN_CAP; ++spins) {
      REREAD();
      ya = ld_sc1(rs, yoff);
      if (all_lanes(tagged(ya, T))) break;
    }
  }
  f32x4 r4 = {fA * bf_lo(ya.x), fA * bf_hi(ya.x), fA * bf_lo(ya.z), fA * bf_hi(ya.z)};
#pragma unroll
  for (int k = 0; k < 4; ++k) { float t_ = r4[k]; t_ += dpp<ROR4>(t_); t_ += dpp<ROR8>(t_); r4[k] = x32_sum(x16_sum(t_)); }
  if (lane < 4) *(LAS f32x4*)(L + L_CRED + (w * 32 + 4 * lane) * 4) = r4;
  __syncthreads();
  if (tid < 16) {
    float sv = 0.f, fv2 = 0.f;
#pragma unroll
    for (int ww = 0; ww < 8; ++ww) {
      sv += *(const LAS float*)(L + L_CRED + (ww * 32 + tid) * 4);
      fv2 += *(const LAS float*)(L + L_CRED + (ww * 32 + 16 + tid) * 4);
    }
    const float t = rbf(rbf(fv2) + bf_lo(b2v));
    const float gt = rbf(t / (1.0f + __expf(-t)));
    const float o = bf_lo(xov) + rbf(rbf(sv) * gt);
    out[bo * D + 16 * ch + tid] = __builtin_bit_cast(unsigned short, (__bf16)o);
  }
  if (uo == 0 && w == 1) {
#pragma nounroll
    for (unsigned spins = 0; spins < SPIN_CAP; ++spins) {
      REREAD();
      const v4u a = ld_sc1(rs, WS_ML + (128 + lane) * 16);
      const v4u d = ld_sc1(rs, WS_ML + (192 + lane) * 16);
      if (all_lanes(tagged(a, T) && tagged(d, T))) break;
      __builtin_amdgcn_s_sleep(1);
    }
    if (lane == 0) __builtin_amdgcn_raw_buffer_store_b32(T, rs, WS_EPOCH, 0, SC1);
  }
}

}

extern "C" void kernel_launch(void* const* d_in, const int* in_sizes, int n_in, void* d_out, int out_size, void* d_ws, size_t ws_size,
                              hipStream_t stream) {
  (void)in_sizes; (void)n_in; (void)out_size; (void)ws_size;
  fused_decode_kernel<<<256, NTHR, 0, stream>>>(
      (const unsigned short*)d_in[0], (const unsigned short*)d_in[1], (const unsigned short*)d_in[2], (const unsigned short*)d_in[3],
      (const unsigned short*)d_in[4], (const unsigned short*)d_in[6], (const unsigned short*)d_in[7], (const unsigned short*)d_in[8],
      (const unsigned short*)d_in[9], (unsigned short*)d_out, (unsigned char*)d_ws);
}
```

```cpp
#include <hip/hip_runtime.h>
#include <stdint.h>
#include <type_traits>

#define GAS __attribute__((address_space(1)))
#define LAS __attribute__((address_space(3)))
typedef unsigned v4u __attribute__((ext_vector_type(4)));
typedef unsigned v2u __attribute__((ext_vector_type(2)));
typedef float f32x4 __attribute__((ext_vector_type(4)));
typedef float f32x2 __attribute__((ext_vector_type(2)));
typedef __bf16 bf16x8 __attribute__((ext_vector_type(8)));
typedef __bf16 bf16x2 __attribute__((ext_vector_type(2)));
typedef __amdgpu_buffer_rsrc_t rsrc_t;

namespace {

constexpr int D = 640, NH = 32, SK = 8192, DH = 128, FF = 1728;
constexpr int NTHR = 512;
constexpr int NS = 6;
constexpr int NCH = 256;
constexpr int PF = 4;
constexpr int NPEEL = 10;
constexpr int E2_EVEN = 8, E2_ODD = 14;
constexpr int WOB = 2;
static_assert((PF + NPEEL + 1 + E2_ODD) * NS <= NCH && E2_EVEN >= 2 && E2_EVEN % 2 == 0 && E2_ODD % 2 == 0, "the fixed tiles fit");
constexpr int NT = 2, PLAIN = 0, SC1 = 16;
constexpr float SCALE = 0.08838834764831845f, LOG2E = 1.4426950408889634f;

constexpr int WS_EPOCH = 0;
constexpr int WS_ML = 4096;
constexpr int WS_F = 65536;
constexpr int WS_Y = 524288;
constexpr int WS_PB = WS_Y + 2 * 40 * 128 * 64;
constexpr int WS_BYTES = WS_PB + 27 * 8 * 1024;
constexpr unsigned SPIN_CAP = 1u << 20;

constexpr int L_XN = 0;
constexpr int L_QRED = 2560;
constexpr int L_ORED = 6656;
constexpr int L_ML = 9728;
constexpr int L_HV = 9792;
constexpr int L_CRED = 10304;
constexpr int L_YRED = 11328;
constexpr int L_BYTES = 31808;

__device__ __forceinline__ float bf_lo(unsigned w) { return __builtin_bit_cast(float, w << 16); }
__device__ __forceinline__ float bf_hi(unsigned w) { return __builtin_bit_cast(float, w & 0xffff0000u); }
__device__ __forceinline__ unsigned pk_bf16(float lo, float hi) {
  bf16x2 v = {(__bf16)lo, (__bf16)hi};
  return __builtin_bit_cast(unsigned, v);
}
__device__ __forceinline__ float rbf(float f) { return (float)(__bf16)f; }
template <int CTRL> __device__ __forceinline__ float dpp(float x) {
  return __builtin_bit_cast(float, __builtin_amdgcn_mov_dpp(__builtin_bit_cast(int, x), CTRL, 0xf, 0xf, true));
}
template <int CTRL> __device__ __forceinline__ unsigned dppu(unsigned x) {
  return (unsigned)__builtin_amdgcn_mov_dpp((int)x, CTRL, 0xf, 0xf, true);
}
constexpr int XOR1 = 0xB1, XOR2 = 0x4E, XOR7 = 0x141, XOR15 = 0x140, ROR4 = 0x124, ROR8 = 0x128;
__device__ __forceinline__ float x16_sum(float x) {
  auto s = __builtin_amdgcn_permlane16_swap(__float_as_uint(x), __float_as_uint(x), false, false);
  return __uint_as_float(s[0]) + __uint_as_float(s[1]);
}
__device__ __forceinline__ float x32_sum(float x) {
  auto s = __builtin_amdgcn_permlane32_swap(__float_as_uint(x), __float_as_uint(x), false, false);
  return __uint_as_float(s[0]) + __uint_as_float(s[1]);
}
__device__ __forceinline__ float x16_max(float x) {
  auto s = __builtin_amdgcn_permlane16_swap(__float_as_uint(x), __float_as_uint(x), false, false);
  return fmaxf(__uint_as_float(s[0]), __uint_as_float(s[1]));
}
__device__ __forceinline__ float x32_max(float x) {
  auto s = __builtin_amdgcn_permlane32_swap(__float_as_uint(x), __float_as_uint(x), false, false);
  return fmaxf(__uint_as_float(s[0]), __uint_as_float(s[1]));
}
__device__ __forceinline__ float wave_sum(float x) {
  x += dpp<XOR1>(x); x += dpp<XOR2>(x); x += dpp<XOR7>(x); x += dpp<XOR15>(x);
  return x32_sum(x16_sum(x));
}
__device__ __forceinline__ float dot2(unsigned a, unsigned b, float acc) {
  return __builtin_amdgcn_fdot2_f32_bf16(__builtin_bit_cast(bf16x2, a), __builtin_bit_cast(bf16x2, b), acc, false);
}
__device__ __forceinline__ f32x4 rowmac(unsigned P, const unsigned (&mk)[4], v4u rows, f32x4 acc) {
  const v4u a = {P & mk[0], P & mk[1], P & mk[2], P & mk[3]};
  return __builtin_amdgcn_mfma_f32_16x16x32_bf16(__builtin_bit_cast(bf16x8, a), __builtin_bit_cast(bf16x8, rows), acc, 0, 0, 0);
}
__device__ __forceinline__ rsrc_t mkrs(const void* p, unsigned bytes) { return __builtin_amdgcn_make_buffer_rsrc((void*)p, 0, bytes, 0x00020000); }
template <int AUX> __device__ __forceinline__ v4u ldx(rsrc_t r, unsigned voff, unsigned soff) { return __builtin_amdgcn_raw_buffer_load_b128(r, voff, soff, AUX); }
__device__ __forceinline__ v4u ld_sc1(rsrc_t r, unsigned off) { return __builtin_amdgcn_raw_buffer_load_b128(r, off, 0, SC1); }
__device__ __forceinline__ void st_sc1(rsrc_t r, unsigned off, v4u v) { __builtin_amdgcn_raw_buffer_store_b128(v, r, off, 0, SC1); }
__device__ __forceinline__ bool tagged(v4u v, unsigned T) { return v.y == T && v.w == T; }
__device__ __forceinline__ bool all_lanes(bool ok) { return __builtin_amdgcn_ballot_w64(!ok) == 0; }
#define SB() __builtin_amdgcn_sched_barrier(0)
#define REREAD() asm volatile("" ::: "memory")
#define DRAIN() do { SB(); __builtin_amdgcn_s_waitcnt(0x0F70); SB(); } while (0)

__device__ __forceinline__ unsigned tile_scores(const v4u (&k)[2], const unsigned (&q)[4], float& m, float& l, f32x4& acc, int lane) {
  float x[2];
#pragma unroll
  for (int i = 0; i < 2; ++i) {
    float d = dot2(k[i].x, q[0], 0.f);
    d = dot2(k[i].y, q[1], d); d = dot2(k[i].z, q[2], d); d = dot2(k[i].w, q[3], d);
    x[i] = d;
  }
  const float a0 = x[0] + dpp<XOR1>(x[0]), a1 = x[1] + dpp<XOR1>(x[1]);
  float s = (lane & 1) ? a1 : a0;
  s += dpp<XOR2>(s);
  s += dpp<ROR4>(s);
  s += dpp<ROR8>(s);
  s = rbf(rbf(s) * SCALE) * LOG2E;
  if (__builtin_amdgcn_ballot_w64(s > m) != 0) {
    float mx = fmaxf(s, dpp<XOR1>(s));
    mx = x32_max(x16_max(mx));
    mx = fmaxf(mx, m);
    const float al = __builtin_amdgcn_exp2f(m - mx);
    l *= al; acc *= al; m = mx;
  }
  const float p = __builtin_amdgcn_exp2f(s - m);
  l += p;
  return pk_bf16(p, p);
}
__device__ __forceinline__ f32x4 tile_pv(unsigned P, const v4u (&v)[2], const unsigned (&mk)[4], f32x4 acc) {
  acc = rowmac(dppu<0x00>(P), mk, v[0], acc);
  acc = rowmac(dppu<0x55>(P), mk, v[1], acc);
  asm volatile("" : "+v"(acc));
  return acc;
}

__global__ void __launch_bounds__(NTHR, 2)
fused_decode_kernel(const unsigned short* __restrict__ x, const unsigned short* __restrict__ kc, const unsigned short* __restrict__ vc,
                    const unsigned short* __restrict__ wq, const unsigned short* __restrict__ wo, const unsigned short* __restrict__ w1,
                    const unsigned short* __restrict__ b1, const unsigned short* __restrict__ w2, const unsigned short* __restrict__ b2,
                    unsigned short* __restrict__ out, unsigned char* __restrict__ ws) {
  asm volatile("" :: "s"(x), "s"(kc), "s"(vc), "s"(wq), "s"(wo), "s"(w1), "s"(b1), "s"(w2), "s"(b2), "s"(out), "s"(ws));
  __shared__ __attribute__((aligned(16))) unsigned char smem[L_BYTES];
  LAS unsigned char* L = (LAS unsigned char*)smem;
  const int tid = threadIdx.x, lane = tid & 63, w = __builtin_amdgcn_readfirstlane(tid >> 6);
  const int c = blockIdx.x, xl = c & 7, ii = c >> 3;
  const int h = xl * 4 + (ii >> 3), j8 = ii & 7, b = j8 >> 2, s = j8 & 3;
  const int c16 = lane & 15, g = lane >> 4;
  const rsrc_t rs = mkrs(ws, WS_BYTES);
  const rsrc_t rK = mkrs(kc, 2u * NH * SK * DH * 2), rV = mkrs(vc, 2u * NH * SK * DH * 2);
  const rsrc_t rQ = mkrs(wq, D * NH * DH * 2), rO = mkrs(wo, D * NH * DH * 2);

  const unsigned epoch = __builtin_amdgcn_raw_buffer_load_b32(rs, WS_EPOCH, 0, SC1);
  unsigned xw[5];
  {
    const GAS unsigned* x32 = (const GAS unsigned*)x + (w >> 2) * 320;
#pragma unroll
    for (int k = 0; k < 5; ++k) xw[k] = x32[k * 64 + lane];
  }
  SB();
  const int G = xl + 8 * (ii >> 3);
  const bool ffn = G < 27;
  const int uo = c >= 176 ? c - 176 : 0, bo = uo / 40, ch = uo - 40 * bo;
  SB();
  const unsigned kvb = ((unsigned)(b * NH + h) * SK + s * 2048) * 256u;
#define KV_OFF(ck) (kvb + (((ck) + 40 * s) & (NCH - 1)) * 2048)
  const unsigned lo16 = lane * 16;
  const int rot = j8 % 5;
  const unsigned wqb = (80 * w) * 8192 + h * 256, wqoff = g * (20 * 8192) + c16 * 16;
  v4u wreg[20];
  v4u pk[PF][2], pv[PF][2];
  unsigned mk[4];
#pragma unroll
  for (int jj = 0; jj < 4; ++jj) mk[jj] = (c16 < 8 && (c16 >> 1) == jj) ? ((c16 & 1) ? 0xffff0000u : 0x0000ffffu) : 0u;
  unsigned T = 0;
#define ROTK(k) ((k) + rot >= 5 ? (k) + rot - 5 : (k) + rot)
#define WQ_GROUP(k) do { const unsigned rb_ = wqb + ROTK(k) * (4 * 8192);                              \
    _Pragma("unroll") for (int i_ = 0; i_ < 4; ++i_) wreg[4 * (k) + i_] = ldx<PLAIN>(rQ, wqoff, rb_ + i_ * 8192); } while (0)
#define K_LOADS(buf, ck) _Pragma("unroll") for (int i_ = 0; i_ < 2; ++i_) pk[buf][i_] = ldx<NT>(rK, lo16 + i_ * 1024, KV_OFF(ck))
#define V_LOADS(buf, ck) _Pragma("unroll") for (int i_ = 0; i_ < 2; ++i_) pv[buf][i_] = ldx<NT>(rV, lo16 + i_ * 1024, KV_OFF(ck))
#define KV_PRE(n) do { if constexpr (ST) { K_LOADS(n, (n) * NS + w); V_LOADS(n, (n) * NS + w); } } while (0)
#define GROUP_END() do { SB(); __builtin_amdgcn_s_barrier(); SB(); } while (0)
  auto q_phase = [&](auto st_, auto helper_first_loads) __attribute__((always_inline)) {
    constexpr bool ST = decltype(st_)::value;
    if constexpr (ST) { WQ_GROUP(0); WQ_GROUP(1); } else helper_first_loads();
    SB();
    {
      asm volatile("" : "+v"(xw[0]), "+v"(xw[1]), "+v"(xw[2]), "+v"(xw[3]), "+v"(xw[4]));
      float v[10];
#pragma unroll
      for (int k = 0; k < 5; ++k) { v[2 * k] = bf_lo(xw[k]); v[2 * k + 1] = bf_hi(xw[k]); }
      const float x0 = __builtin_bit_cast(float, __builtin_amdgcn_readfirstlane(__builtin_bit_cast(int, v[0])));
      float s1 = 0.f, s2 = 0.f;
#pragma unroll
      for (int k = 0; k < 10; ++k) { const float d = v[k] - x0; s1 += d; s2 = fmaf(d, d, s2); }
      s1 = wave_sum(s1) * (1.0f / D); s2 = wave_sum(s2) * (1.0f / D);
      const float mean = x0 + s1, var = fmaxf(fmaf(-s1, s1, s2), 0.f);
      const float mb = rbf(mean);
      const float den = rbf(__builtin_amdgcn_sqrtf(rbf(rbf(var) + 1e-5f)));
      const float inv = __builtin_amdgcn_rcpf(den);
      LAS unsigned* xn32 = (LAS unsigned*)(L + L_XN) + (w >> 2) * 320;
#pragma unroll
      for (int k = 0; k < 5; ++k) xn32[k * 64 + lane] = pk_bf16(rbf(v[2 * k] - mb) * inv, rbf(v[2 * k + 1] - mb) * inv);
      asm volatile("s_waitcnt lgkmcnt(0)" ::: "memory");
    }
    GROUP_END();
    KV_PRE(0); KV_PRE(1);
    if constexpr (!ST) { WQ_GROUP(0); WQ_GROUP(1); }
    WQ_GROUP(2); WQ_GROUP(3); WQ_GROUP(4);
    GROUP_END();
    static_assert(PF == 4, "prologue issue order");
    KV_PRE(2); KV_PRE(3);
    SB();
    T = __builtin_amdgcn_readfirstlane(epoch) + 1u;
    const LAS v2u* xp = (const LAS v2u*)(L + L_XN + b * 1280 + (80 * w + 20 * g) * 2);
    f32x4 qa = {0.f, 0.f, 0.f, 0.f}, qb = {0.f, 0.f, 0.f, 0.f};
#pragma unroll
    for (int k = 0; k < 5; ++k) {
      const v2u r = xp[ROTK(k)];
      qa = rowmac(__builtin_amdgcn_perm(r.x, r.x, 0x01000100u), mk, wreg[4 * k], qa);
      qb = rowmac(__builtin_amdgcn_perm(r.x, r.x, 0x03020302u), mk, wreg[4 * k + 1], qb);
      qa = rowmac(__builtin_amdgcn_perm(r.y, r.y, 0x01000100u), mk, wreg[4 * k + 2], qa);
      qb = rowmac(__builtin_amdgcn_perm(r.y, r.y, 0x03020302u), mk, wreg[4 * k + 3], qb);
    }
    qa += qb;
    if (lane < 32) *(LAS f32x4*)(L + L_QRED + (w * 128 + 8 * c16 + 4 * g) * 4) = qa;
  };

  const unsigned wob = (h * 128 + 16 * w) * 1280;
  const unsigned wooff = g * (4 * 1280) + c16 * 16;
#define WO_LOAD(n) wreg[n] = ldx<PLAIN>(rO, wooff + ((n) % 5) * 256, wob + ((n) / 5) * 1280)
  f32x4 fo[2] = {{0.f, 0.f, 0.f, 0.f}, {0.f, 0.f, 0.f, 0.f}};

  if (w < NS) {
    q_phase(std::true_type{}, [] {});
    __syncthreads();
    unsigned q[4];
    {
      const LAS f32x4* qr = (const LAS f32x4*)(L + L_QRED + ((2 * g) * 128 + 8 * c16) * 4);
      f32x4 lo = qr[0] + qr[32], hi = qr[1] + qr[33];
      float e[8] = {lo.x, lo.y, lo.z, lo.w, hi.x, hi.y, hi.z, hi.w};
#pragma unroll
      for (int k = 0; k < 8; ++k) e[k] = x32_sum(x16_sum(e[k]));
#pragma unroll
      for (int k = 0; k < 4; ++k) q[k] = pk_bf16(e[2 * k], e[2 * k + 1]);
    }
    SB();
    float m = -INFINITY, l = 0.f;
    f32x4 acc = {0.f, 0.f, 0.f, 0.f};
    unsigned nx = PF * NS + w;
#define TILE_STEP(buf, EXTRA) do { const unsigned P_ = tile_scores(pk[buf], q, m, l, acc, lane); SB(); K_LOADS(buf, nx); EXTRA; SB();  \
    acc = tile_pv(P_, pv[buf], mk, acc); SB(); V_LOADS(buf, nx); SB(); nx += NS; } while (0)
#define TILE_LAST(buf) do { const unsigned P_ = tile_scores(pk[buf], q, m, l, acc, lane); acc = tile_pv(P_, pv[buf], mk, acc); SB(); } while (0)
    TILE_STEP(0, );
#pragma unroll
    for (int u = 1; u < PF; ++u) TILE_LAST(u);
#pragma unroll
    for (int u = 0; u < NPEEL; ++u) TILE_STEP(0, WO_LOAD(2 * u); WO_LOAD(2 * u + 1));
    const int e2 = (__builtin_amdgcn_s_getreg(20 | (3 << 11)) & 1) ? E2_ODD : E2_EVEN;
    const unsigned nlim = ((NCH - w + NS - 1) / NS - (e2 - 1)) * NS + w;
#pragma nounroll
    while (nx < nlim) TILE_STEP(0, );
    K_LOADS(1, nx); V_LOADS(1, nx);
    SB();
    nx += NS;
#pragma nounroll
    for (int j = 0; j < e2 / 2 - 1; ++j) { TILE_STEP(0, ); TILE_STEP(1, ); }
    TILE_LAST(0);
    TILE_LAST(1);
#undef TILE_STEP
#undef TILE_LAST
    const float lw = x32_sum(x16_sum(l + dpp<XOR1>(l)));
    if (lane < 32) *(LAS f32x4*)(L + L_ORED + (w * 128 + 8 * c16 + 4 * g) * 4) = acc;
    if (lane == 0) *(LAS f32x2*)(L + L_ML + w * 8) = f32x2{m, lw};
  } else {
    v4u fw[16];
    const bool hA = w == NS;
    const rsrc_t rF = mkrs(hA ? w1 : w2, ffn ? D * FF * 2 : 0), rF2 = mkrs(w2, ffn && !hA ? D * FF * 2 : 0);
    const unsigned fvoff = hA ? (10 * (2 * g + (c16 >> 3))) * 3456 + (c16 & 7) * 16 : g * (16 * 1280) + (c16 < 10 ? c16 : 9) * 16;
    const unsigned fsoff = hA ? (80 * j8) * 3456 + 128 * G : (64 * G) * 1280 + 160 * j8;
    q_phase(std::false_type{}, [&]() __attribute__((always_inline)) {
#pragma unroll
      for (int k = 0; k < 16; ++k) fw[k] = ldx<PLAIN>(k < 10 ? rF : rF2, fvoff, fsoff + (hA ? k * 3456 : k * 1280));
    });
    __syncthreads();
    const int fu = 8 * (lane & 7) + 4 * ((lane >> 3) & 1) + 2 * ((lane >> 4) & 1);
    const unsigned b1v = ((const GAS unsigned*)b1)[(ffn ? 32 * G : 0) + (fu >> 1)];
    if (hA && ffn) {
      unsigned mk2[4];
#pragma unroll
      for (int jj = 0; jj < 4; ++jj) mk2[jj] = ((c16 & 7) >> 1) == jj ? ((c16 & 1) ? 0xffff0000u : 0x0000ffffu) : 0u;
      const LAS unsigned* xs = (const LAS unsigned*)(L + L_XN + 160 * j8 + 20 * (2 * g + (c16 >> 3)));
      f32x4 d0 = {0.f, 0.f, 0.f, 0.f}, d1 = {0.f, 0.f, 0.f, 0.f};
#pragma unroll
      for (int k = 0; k < 10; ++k) {
        const unsigned x0_ = xs[k >> 1], x1_ = xs[320 + (k >> 1)], sel = (k & 1) ? 0x03020302u : 0x01000100u;
        d0 = rowmac(__builtin_amdgcn_perm(x0_, x0_, sel), mk2, fw[k], d0);
        d1 = rowmac(__builtin_amdgcn_perm(x1_, x1_, sel), mk2, fw[k], d1);
      }
#pragma unroll
      for (int bb = 0; bb < 2; ++bb) {
        f32x4 d = bb ? d1 : d0;
#pragma unroll
        for (int r = 0; r < 4; ++r) {
          auto sw = __builtin_amdgcn_permlane32_swap(__float_as_uint(d[r]), __float_as_uint(d[r]), false, false);
          d[r] += dpp<ROR8>(__uint_as_float(sw[1]));
        }
        const unsigned o = WS_PB + (G * 8 + j8) * 1024 + bb * 512 + (c16 + 8 * g) * 16;
        if (lane < 32 && c16 < 8) {
          st_sc1(rs, o, v4u{__float_as_uint(d.x), T, __float_as_uint(d.y), T});
          st_sc1(rs, o + 256, v4u{__float_as_uint(d.z), T, __float_as_uint(d.w), T});
        }
      }
    }
#pragma unroll
    for (int n = 0; n < 20; n += WOB) {
#pragma unroll
      for (int k = n; k < n + WOB; ++k) WO_LOAD(k);
      DRAIN();
    }
    if (!hA && ffn) {
      v4u pg[8];
#pragma nounroll
      for (unsigned spins = 0; spins < SPIN_CAP; ++spins) {
        REREAD();
        bool ok = true;
#pragma unroll
        for (int k = 0; k < 8; ++k) pg[k] = ld_sc1(rs, WS_PB + (G * 8 + k) * 1024 + lane * 16);
#pragma unroll
        for (int k = 0; k < 8; ++k) ok = ok && tagged(pg[k], T);
        if (all_lanes(ok)) break;
        __builtin_amdgcn_s_sleep(32);
      }
      float a0 = 0.f, a1 = 0.f;
#pragma unroll
      for (int k = 0; k < 8; ++k) { a0 += __uint_as_float(pg[k].x); a1 += __uint_as_float(pg[k].z); }
      const float r0 = fmaxf(rbf(rbf(a0) + bf_lo(b1v)), 0.f), r1 = fmaxf(rbf(rbf(a1) + bf_hi(b1v)), 0.f);
      const float h0 = r0 * r0, h1 = r1 * r1;
      *(LAS v2u*)(L + L_HV + ((lane >> 5) * 64 + fu) * 4) = v2u{pk_bf16(h0, h0), pk_bf16(h1, h1)};
      asm volatile("s_waitcnt lgkmcnt(0)" ::: "memory");
#pragma unroll
      for (int bb = 0; bb < 2; ++bb) {
        f32x4 da = {0.f, 0.f, 0.f, 0.f}, db = {0.f, 0.f, 0.f, 0.f};
#pragma unroll
        for (int k = 0; k < 4; ++k) {
          const v4u hv = *(const LAS v4u*)(L + L_HV + (bb * 64 + 16 * g + 4 * k) * 4);
          da = rowmac(hv.x, mk, fw[4 * k], da); db = rowmac(hv.y, mk, fw[4 * k + 1], db);
          da = rowmac(hv.z, mk, fw[4 * k + 2], da); db = rowmac(hv.w, mk, fw[4 * k + 3], db);
        }
        fo[bb] = da + db;
      }
    }
  }
#undef WO_LOAD

  DRAIN();
  __syncthreads();
  if (w == NS + 1 && ffn && lane < 32 && c16 < 10) {
#pragma unroll
    for (int bb = 0; bb < 2; ++bb) {
      const unsigned o = WS_F + ((bb * 40 + 5 * j8 + (c16 >> 1)) * 32 + G) * 128 + (2 * (c16 & 1) + g) * 16;
      st_sc1(rs, o, v4u{__float_as_uint(fo[bb].x), T, __float_as_uint(fo[bb].y), T});
      st_sc1(rs, o + 64, v4u{__float_as_uint(fo[bb].z), T, __float_as_uint(fo[bb].w), T});
    }
  }
  const int p = h * 4 + s;
  {
    f32x2 ml[NS];
    f32x4 ov[NS];
#pragma unroll
    for (int ww = 0; ww < NS; ++ww) ml[ww] = *(const LAS f32x2*)(L + L_ML + ww * 8);
#pragma unroll
    for (int ww = 0; ww < NS; ++ww) ov[ww] = *(const LAS f32x4*)(L + L_ORED + (ww * 128 + 16 * w + 4 * g) * 4);
    SB();
    float M = ml[0].x;
#pragma unroll
    for (int ww = 1; ww < NS; ++ww) M = fmaxf(M, ml[ww].x);
    float Ls = 0.f;
    f32x4 o = {0.f, 0.f, 0.f, 0.f};
#pragma unroll
    for (int ww = 0; ww < NS; ++ww) {
      const float f = __builtin_amdgcn_exp2f(ml[ww].x - M);
      Ls = fmaf(f, ml[ww].y, Ls);
      o += f * ov[ww];
    }
    if (tid == 448) st_sc1(rs, WS_ML + (b * 128 + p) * 16, v4u{__float_as_uint(M), T, __float_as_uint(Ls), T});
    f32x4 y[5];
#pragma unroll
    for (int cc = 0; cc < 5; ++cc) y[cc] = f32x4{0.f, 0.f, 0.f, 0.f};
#pragma unroll
    for (int i = 0; i < 4; ++i) {
      const unsigned P = pk_bf16(o[i], o[i]);
#pragma unroll
      for (int cc = 0; cc < 5; ++cc) y[cc] = rowmac(P, mk, wreg[i * 5 + cc], y[cc]);
    }
    if (lane < 32) {
#pragma unroll
      for (int cc = 0; cc < 5; ++cc) *(LAS f32x4*)(L + L_YRED + (w * 640 + cc * 128 + 8 * c16 + 4 * g) * 4) = y[cc];
    }
  }
  __syncthreads();
  if (tid < 160) {
    f32x4 a = {0.f, 0.f, 0.f, 0.f};
#pragma unroll
    for (int ww = 0; ww < 8; ++ww) a += *(const LAS f32x4*)(L + L_YRED + (ww * 640 + 4 * tid) * 4);
    st_sc1(rs, WS_Y + ((b * 40 + (tid >> 2)) * 128 + p) * 64 + (tid & 3) * 16, v4u{pk_bf16(a.x, a.y), T, pk_bf16(a.z, a.w), T});
  }
  if (c < 176) return;

  const unsigned short xov = x[bo * D + 16 * ch + c16], b2v = b2[16 * ch + c16];
  SB();
  const int pa = 16 * w + (lane >> 2), pc = lane & 7, kf = 4 * w + (lane >> 3);
  const bool fv = lane < 32 && kf < 27;
  const unsigned foff = WS_F + ((bo * 40 + ch) * 32 + (fv ? kf : 0)) * 128 + pc * 16;
  v4u ma, fa;
#pragma nounroll
  for (unsigned spins = 0; spins < SPIN_CAP; ++spins) {
    REREAD();
    ma = ld_sc1(rs, WS_ML + (bo * 128 + pa) * 16);
    fa = ld_sc1(rs, foff);
    if (all_lanes(tagged(ma, T) && tagged(fa, T))) break;
    __builtin_amdgcn_s_sleep(1);
  }
  const unsigned yoff = WS_Y + ((bo * 40 + ch) * 128 + pa) * 64 + (lane & 3) * 16;
  v4u ya = ld_sc1(rs, yoff);
  SB();
  float fA;
  {
    const float m_ = __uint_as_float(ma.x), l_ = __uint_as_float(ma.z);
    float M = fmaxf(m_, dpp<ROR4>(m_));
    M = fmaxf(M, dpp<ROR8>(M));
    const float e = __builtin_amdgcn_exp2f(m_ - M);
    float Ls = e * l_;
    Ls += dpp<ROR4>(Ls);
    Ls += dpp<ROR8>(Ls);
    fA = e / Ls;
  }
  float r2[2] = {fv ? __uint_as_float(fa.x) : 0.f, fv ? __uint_as_float(fa.z) : 0.f};
#pragma unroll
  for (int k = 0; k < 2; ++k) { r2[k] += dpp<ROR8>(r2[k]); r2[k] = x32_sum(x16_sum(r2[k])); }
  if (lane < 8) *(LAS f32x2*)(L + L_CRED + (w * 32 + 16 + 4 * (pc & 3) + 2 * (pc >> 2)) * 4) = f32x2{r2[0], r2[1]};
  SB();
  if (!all_lanes(tagged(ya, T))) {
#pragma nounroll
    for (unsigned spins = 0; spins < SPIN_CAP; ++spins) {
      REREAD();
      ya = ld_sc1(rs, yoff);
      if (all_lanes(tagged(ya, T))) break;
    }
  }
  f32x4 r4 = {fA * bf_lo(ya.x), fA * bf_hi(ya.x), fA * bf_lo(ya.z), fA * bf_hi(ya.z)};
#pragma unroll
  for (int k = 0; k < 4; ++k) { float t_ = r4[k]; t_ += dpp<ROR4>(t_); t_ += dpp<ROR8>(t_); r4[k] = x32_sum(x16_sum(t_)); }
  if (lane < 4) *(LAS f32x4*)(L + L_CRED + (w * 32 + 4 * lane) * 4) = r4;
  __syncthreads();
  if (tid < 16) {
    float sv = 0.f, fv2 = 0.f;
#pragma unroll
    for (int ww = 0; ww < 8; ++ww) {
      sv += *(const LAS float*)(L + L_CRED + (ww * 32 + tid) * 4);
      fv2 += *(const LAS float*)(L + L_CRED + (ww * 32 + 16 + tid) * 4);
    }
    const float t = rbf(rbf(fv2) + bf_lo(b2v));
    const float gt = rbf(t / (1.0f + __expf(-t)));
    const float o = bf_lo(xov) + rbf(rbf(sv) * gt);
    out[bo * D + 16 * ch + tid] = __builtin_bit_cast(unsigned short, (__bf16)o);
  }
  if (uo == 0 && w == 1) {
#pragma nounroll
    for (unsigned spins = 0; spins < SPIN_CAP; ++spins) {
      REREAD();
      const v4u a = ld_sc1(rs, WS_ML + (128 + lane) * 16);
      const v4u d = ld_sc1(rs, WS_ML + (192 + lane) * 16);
      if (all_lanes(tagged(a, T) && tagged(d, T))) break;
      __builtin_amdgcn_s_sleep(1);
    }
    if (lane == 0) __builtin_amdgcn_raw_buffer_store_b32(T, rs, WS_EPOCH, 0, SC1);
  }
}

}

extern "C" void kernel_launch(void* const* d_in, const int* in_sizes, int n_in, void* d_out, int out_size, void* d_ws, size_t ws_size,
                              hipStream_t stream) {
  (void)in_sizes; (void)n_in; (void)out_size; (void)ws_size;
  fused_decode_kernel<<<256, NTHR, 0, stream>>>(
      (const unsigned short*)d_in[0], (const unsigned short*)d_in[1], (const unsigned short*)d_in[2], (const unsigned short*)d_in[3],
      (const unsigned short*)d_in[4], (const unsigned short*)d_in[6], (const unsigned short*)d_in[7], (const unsigned short*)d_in[8],
      (const unsigned short*)d_in[9], (unsigned short*)d_out, (unsigned char*)d_ws);
}
```
